```python
import torch

def _softmax(x, axis=-1):
    return torch.softmax(x, dim=axis)
def _gelu(x):
    return torch.nn.functional.gelu(x, approximate="tanh")
def _silu(x):
    return torch.nn.functional.silu(x)
def _sigmoid(x):
    return torch.sigmoid(x)
def _layernorm(x, axis=-1, eps=1e-5):
    m = x.mean(dim=axis, keepdim=True)
    v = x.var(dim=axis, keepdim=True, unbiased=False)
    return (x - m) / torch.sqrt(v + eps)
def _scatter_add(tgt, idx, src, axis=0):
    return tgt.clone().index_add_(axis, idx, src)
def _segment_sum(src, seg, n):
    out = torch.zeros((n,) + src.shape[1:], dtype=src.dtype)
    return out.index_add_(0, seg, src)
def _pad(x, pw):
    flat = tuple(v for pair in reversed(pw) for v in pair)
    return torch.nn.functional.pad(x, flat)

kernel_name = 'standalone__batchnorm__sampling'

def setup_inputs():

    _w = (lambda a: (lambda t: t.to(torch.bfloat16) if t.is_floating_point() else t)(torch.as_tensor(a)))
    B, S, C = 1024, 4, 576
    K = 3
    return {
        "x": _w((lambda *s: torch.randn(*s, dtype=torch.bfloat16) * 0.05)(B, S, C)),
        "w": _w((lambda *s: torch.randn(*s, dtype=torch.bfloat16) * 0.05)(K, C, C) * (1.0 / (K * C) ** 0.5)),
        "mean": _w((lambda *s: torch.randn(*s, dtype=torch.bfloat16) * 0.05)(C) * 0.1),
        "var": _w(torch.abs((lambda *s: torch.randn(*s, dtype=torch.bfloat16) * 0.05)(C)) + 0.5),
        "g": _w((lambda *s: torch.randn(*s, dtype=torch.bfloat16) * 0.05)(C) + 1.0),
        "b": _w((lambda *s: torch.randn(*s, dtype=torch.bfloat16) * 0.05)(C) * 0.1),
    }

def reference(x, w, mean, var, g, b):
    K = 3
    P = K // 2
    S = x.shape[1]
    xp = _pad(x, ((0, 0), (P, P), (0, 0)))
    y = sum(torch.einsum('bsc,cd->bsd', xp[:, k:k + S, :], w[k]) for k in range(K))
    inv_std = 1.0 / torch.sqrt(var + 1e-5)
    y = (y - mean) * (g * inv_std) + b
    return _silu(y)
```

```cpp
#include <hip/hip_runtime.h>
#include <hip/hip_bf16.h>

__global__ void naive_kernel(const __hip_bfloat16* x, const __hip_bfloat16* w, const __hip_bfloat16* mean,
                             const __hip_bfloat16* var, const __hip_bfloat16* g, const __hip_bfloat16* b,
                             __hip_bfloat16* out) {
    const int C = 576, S = 4;
    int row = blockIdx.x;
    int d = threadIdx.x;
    int bi = row / S, s = row % S;
    float acc = 0.f;
    for (int k = 0; k < 3; ++k) {
        int sp = s + k - 1;
        if (sp < 0 || sp >= S) continue;
        const __hip_bfloat16* xr = x + (size_t)(bi * S + sp) * C;
        const __hip_bfloat16* wk = w + (size_t)k * C * C + d;
        for (int c = 0; c < C; ++c) acc += __bfloat162float(xr[c]) * __bfloat162float(wk[(size_t)c * C]);
    }
    float inv = 1.0f / sqrtf(__bfloat162float(var[d]) + 1e-5f);
    float y = (acc - __bfloat162float(mean[d])) * (__bfloat162float(g[d]) * inv) + __bfloat162float(b[d]);
    out[(size_t)row * C + d] = __float2bfloat16(y / (1.f + __expf(-y)));
}

extern "C" void kernel_launch(void* const* d_in, const int* in_sizes, int n_in,
                              void* d_out, int out_size, void* d_ws, size_t ws_size,
                              hipStream_t stream) {
    naive_kernel<<<4096, 576, 0, stream>>>((const __hip_bfloat16*)d_in[0], (const __hip_bfloat16*)d_in[1],
        (const __hip_bfloat16*)d_in[2], (const __hip_bfloat16*)d_in[3], (const __hip_bfloat16*)d_in[4],
        (const __hip_bfloat16*)d_in[5], (__hip_bfloat16*)d_out);
}
```

```cpp
#include <hip/hip_runtime.h>
#include <hip/hip_bf16.h>

typedef __attribute__((ext_vector_type(4))) __bf16 bf16x4;
typedef __attribute__((ext_vector_type(8))) __bf16 bf16x8;
typedef __attribute__((ext_vector_type(2))) __bf16 bf16x2;
typedef __attribute__((ext_vector_type(4))) float f32x4;
typedef __attribute__((ext_vector_type(4))) unsigned u32x4;
typedef __attribute__((ext_vector_type(2))) unsigned u32x2;
typedef __attribute__((address_space(3))) char lds_char;

constexpr int C = 576, ROWB = C * 2, NB = 40, NBATCH = 1024, NMT = 26, NCT = 9, NWG = NMT * NCT;
constexpr int XB = 172 * 128, WB = 3 * 64 * 128, SLOT = XB + WB, NSTAGE = 9;
constexpr int LDS_BYTES = 3 * SLOT;

__device__ __forceinline__ void dma16(__amdgpu_buffer_rsrc_t rs, unsigned voff, unsigned soff, unsigned ldsaddr) {
    asm volatile("s_mov_b32 m0, %3\n\ts_nop 0\n\tbuffer_load_dwordx4 %0, %1, %2 offen lds"
                 :: "v"(voff), "s"(rs), "s"(soff), "s"(ldsaddr) : "memory");
}

__device__ __forceinline__ float bf2f(unsigned short v) { return __uint_as_float((unsigned)v << 16); }

#define ROT 1

__global__ __launch_bounds__(512, 2) void conv_bn_silu(const char* __restrict__ x, const char* __restrict__ w,
        const char* __restrict__ mean, const char* __restrict__ var, const char* __restrict__ gam,
        const char* __restrict__ beta, char* __restrict__ out) {
    __shared__ __attribute__((aligned(256))) char shm[LDS_BYTES];
    lds_char* lds = (lds_char*)shm;
    const int lane = threadIdx.x & 63;
    const int wid = __builtin_amdgcn_readfirstlane(threadIdx.x >> 6);
    const int xcd = blockIdx.x & 7, idx = blockIdx.x >> 3;
    const int lin = xcd * 29 + (xcd < 2 ? xcd : 2) + idx;
    const int mt = lin / NCT, ct = lin - mt * NCT;
    const int b0 = min(mt * NB, NBATCH - NB);
    const int rot = ROT ? 3 * ((mt + ct) % 3) : 0;

    if (wid >= 4) {
        const int lw = wid - 4;
        auto rx = __builtin_amdgcn_make_buffer_rsrc((void*)x, 0, NBATCH * 4 * ROWB, 0x00020000);
        auto rw = __builtin_amdgcn_make_buffer_rsrc((void*)w, 0, 3 * C * ROWB, 0x00020000);
        unsigned vx[5], lx[5], vw[6], lwo[6];
#pragma unroll
        for (int n = 0; n < 5; ++n) {
            const int t = lw * 5 + n;
            int rho0, grow;
            if (t < 16) {
                rho0 = t * 8;
                grow = (b0 + (t >> 3) * 16 + (t & 1) * 8 + (lane >> 3)) * 4 + ((t >> 1) & 3);
            } else {
                const int e = (t - 16) >> 1, i = (t & 1) * 8 + (lane >> 3);
                rho0 = 132 + e * 20 + (t & 1) * 8;
                grow = (b0 + 32 + e * 4 + (i & 3)) * 4 + (i >> 2);
            }
            const int rho = rho0 + (lane >> 3);
            vx[n] = grow * ROWB + (((lane & 7) ^ ((rho >> 1) & 7)) << 4);
            lx[n] = rho0 * 128;
        }
#pragma unroll
        for (int n = 0; n < 6; ++n) {
            const int u = lw * 6 + n, k = u >> 3, rb = u & 7;
            const int g = ((lane >> 4) & 1) | ((rb & 1) << 1);
            vw[n] = (k * C + rb * 8 + (lane >> 3)) * ROWB + ct * 128 + (((lane & 7) ^ (g << 1)) << 4);
            lwo[n] = XB + u * 1024;
        }
        auto issue = [&](int st, int slot) {
            int ks = st + rot; if (ks >= NSTAGE) ks -= NSTAGE;
            const unsigned base = (unsigned)(size_t)lds + slot * SLOT;
#pragma unroll
            for (int n = 0; n < 5; ++n) dma16(rx, vx[n], ks * 128, base + lx[n]);
#pragma unroll
            for (int n = 0; n < 6; ++n) dma16(rw, vw[n], ks * 64 * ROWB, base + lwo[n]);
        };
        issue(0, 0); issue(1, 1); issue(2, 2);
        asm volatile("s_waitcnt vmcnt(22)\n\ts_barrier" ::: "memory");
        int slot = 0;
        for (int s = 1; s < 7; ++s) {
            asm volatile("s_waitcnt vmcnt(11)\n\ts_barrier" ::: "memory");
            issue(s + 2, slot);
            slot = slot == 2 ? 0 : slot + 1;
        }
        asm volatile("s_waitcnt vmcnt(11)\n\ts_barrier\n\ts_waitcnt vmcnt(0)\n\ts_barrier" ::: "memory");
        return;
    }

    const int j = wid & 1, h = wid >> 1;
    const int i = lane & 15, q = lane >> 4;
    if (threadIdx.x < 96) {
        const int z = threadIdx.x >> 5, o = (threadIdx.x & 31) * 16;
#pragma unroll
        for (int s = 0; s < 3; ++s)
            *(__attribute__((address_space(3))) u32x4*)(lds + s * SLOT + (128 + z * 20) * 128 + o) = u32x4{0, 0, 0, 0};
    }
    float sc[2][4], sh[2][4];
#pragma unroll
    for (int u = 0; u < 2; ++u) {
        const int d = ct * 64 + j * 32 + u * 16 + q * 4;
        const u32x2 m = *(const u32x2*)(mean + d * 2), v = *(const u32x2*)(var + d * 2);
        const u32x2 g = *(const u32x2*)(gam + d * 2), b = *(const u32x2*)(beta + d * 2);
#pragma unroll
        for (int r = 0; r < 4; ++r) {
            const int sft = (r & 1) * 16;
            const float mf = bf2f(m[r >> 1] >> sft), vf = bf2f(v[r >> 1] >> sft);
            const float gf = bf2f(g[r >> 1] >> sft), bf = bf2f(b[r >> 1] >> sft);
            sc[u][r] = gf / sqrtf(vf + 1e-5f);
            sh[u][r] = bf - mf * sc[u][r];
        }
    }
    unsigned ax[2], ae[2][3], aw[2];
#pragma unroll
    for (int ks = 0; ks < 2; ++ks) {
        ax[ks] = h * 8192 + i * 128 + (((4 * ks + q) ^ (i >> 1)) << 4);
#pragma unroll
        for (int k = 0; k < 3; ++k) {
            const int rho = 132 + h * 20 + i + (k - 1) * 4;
            ae[ks][k] = rho * 128 + (((4 * ks + q) ^ ((rho >> 1) & 7)) << 4);
        }
    }
    {
        const int qq = (lane >> 2) & 3, p = lane & 3, g = (qq >> 1) | ((q & 1) << 1);
#pragma unroll
        for (int u = 0; u < 2; ++u)
            aw[u] = XB + (8 * q + qq) * 128 + (((4 * j + 2 * u + (p >> 1)) ^ (g << 1)) << 4) + 8 * (p & 1);
    }
    f32x4 acc[5][2];
#pragma unroll
    for (int t = 0; t < 5; ++t) { acc[t][0] = f32x4{0, 0, 0, 0}; acc[t][1] = f32x4{0, 0, 0, 0}; }

    struct Frags { bf16x8 xm[4], xe[3], wf[3][2]; };
    auto load = [&](Frags& f, int ks, unsigned sb) {
#pragma unroll
        for (int k = 0; k < 3; ++k)
#pragma unroll
            for (int u = 0; u < 2; ++u) {
                lds_char* p = lds + sb + aw[u] + k * 8192 + ks * 4096;
                bf16x4 lo = __builtin_amdgcn_ds_read_tr16_b64_v4bf16((__attribute__((address_space(3))) bf16x4*)p);
                bf16x4 hi = __builtin_amdgcn_ds_read_tr16_b64_v4bf16((__attribute__((address_space(3))) bf16x4*)(p + 512));
                f.wf[k][u] = __builtin_shufflevector(lo, hi, 0, 1, 2, 3, 4, 5, 6, 7);
            }
#pragma unroll
        for (int s = 0; s < 4; ++s) f.xm[s] = *(__attribute__((address_space(3))) bf16x8*)(lds + sb + ax[ks] + s * 2048);
#pragma unroll
        for (int k = 0; k < 3; ++k) f.xe[k] = *(__attribute__((address_space(3))) bf16x8*)(lds + sb + ae[ks][k]);
    };
    auto mma = [&](const Frags& f) {
#pragma unroll
        for (int k = 0; k < 3; ++k)
#pragma unroll
            for (int u = 0; u < 2; ++u) {
#pragma unroll
                for (int s = 0; s < 4; ++s) {
                    const int sp = s + k - 1;
                    if (sp >= 0 && sp < 4) acc[s][u] = __builtin_amdgcn_mfma_f32_16x16x32_bf16(f.wf[k][u], f.xm[sp], acc[s][u], 0, 0, 0);
                }
                acc[4][u] = __builtin_amdgcn_mfma_f32_16x16x32_bf16(f.wf[k][u], f.xe[k], acc[4][u], 0, 0, 0);
            }
    };
    auto interleave = [&]() {
#pragma unroll
        for (int n = 0; n < 19; ++n) {
            __builtin_amdgcn_sched_group_barrier(0x008, 1, 0);
            __builtin_amdgcn_sched_group_barrier(0x100, 1, 0);
        }
        __builtin_amdgcn_sched_group_barrier(0x008, 7, 0);
    };
    Frags P, Q;
    asm volatile("s_waitcnt lgkmcnt(0)\n\ts_barrier" ::: "memory");
    unsigned sb = 0;
    load(P, 0, sb);
    __builtin_amdgcn_sched_barrier(0);
#pragma unroll 1
    for (int s = 0; s < NSTAGE - 1; ++s) {
        load(Q, 1, sb);
        mma(P);
        interleave();
        __builtin_amdgcn_sched_barrier(0);
        asm volatile("s_waitcnt lgkmcnt(0)\n\ts_barrier" ::: "memory");
        __builtin_amdgcn_sched_barrier(0);
        sb = sb == 2 * SLOT ? 0 : sb + SLOT;
        load(P, 0, sb);
        mma(Q);
        interleave();
        __builtin_amdgcn_sched_barrier(0);
    }
    load(Q, 1, sb);
    mma(P);
    interleave();
    __builtin_amdgcn_sched_barrier(0);
    mma(Q);

    const int cofs = (ct * 64 + j * 32 + ((q & 1) << 4) + ((q >> 1) << 3)) * 2;
#pragma unroll
    for (int t = 0; t < 5; ++t) {
        unsigned pk[2][2];
#pragma unroll
        for (int u = 0; u < 2; ++u) {
            float o[4];
#pragma unroll
            for (int r = 0; r < 4; ++r) {
                const float y = acc[t][u][r] * sc[u][r] + sh[u][r];
                o[r] = y * __builtin_amdgcn_rcpf(1.0f + __builtin_amdgcn_exp2f(y * -1.44269504088896340736f));
            }
#pragma unroll
            for (int r = 0; r < 2; ++r) {
                bf16x2 v = {(__bf16)o[2 * r], (__bf16)o[2 * r + 1]};
                pk[u][r] = __builtin_bit_cast(unsigned, v);
            }
        }
        auto s0 = __builtin_amdgcn_permlane16_swap(pk[0][0], pk[1][0], false, false);
        auto s1 = __builtin_amdgcn_permlane16_swap(pk[0][1], pk[1][1], false, false);
        const int row = t < 4 ? (b0 + 16 * h + i) * 4 + t : (b0 + 32 + 4 * h + (i & 3)) * 4 + (i >> 2);
        *(u32x4*)(out + (size_t)row * ROWB + cofs) = u32x4{s0[0], s1[0], s0[1], s1[1]};
    }
}

extern "C" void kernel_launch(void* const* d_in, const int* in_sizes, int n_in,
                              void* d_out, int out_size, void* d_ws, size_t ws_size,
                              hipStream_t stream) {
    conv_bn_silu<<<NWG, 512, 0, stream>>>((const char*)d_in[0], (const char*)d_in[1], (const char*)d_in[2],
        (const char*)d_in[3], (const char*)d_in[4], (const char*)d_in[5], (char*)d_out);
}
```

```cpp
#include <hip/hip_runtime.h>

__global__ __launch_bounds__(512, 2) void conv_bn_silu(const void* x, const void* w, const void* mean, const void* var,
                                                       const void* gam, const void* beta, void* out) {
    __shared__ __attribute__((aligned(256))) char shm[163840];
    asm volatile(
        "L_entry%=:\n"
        "s_load_dwordx4 s[4:7], %0, 0x0\n"
        "s_load_dwordx8 s[8:15], %0, 0x10\n"
        "s_load_dwordx2 s[16:17], %0, 0x30\n"
        "v_lshrrev_b32 v1, 6, %2\n"
        "s_sub_u32 s19, %1, 22\n"
        "v_readfirstlane_b32 s44, v1\n"
        "s_cbranch_scc1 L_pf%=\n"
        "v_and_b32 v1, 63, %2\n"
        "s_and_b32 s18, s19, 7\n"
        "s_mov_b32 s31, s19\n"
        "s_lshr_b32 s19, s19, 3\n"
        "s_and_b32 s38, s44, 3\n"
        "s_cmp_lt_u32 s44, 4\n"
        "s_cbranch_scc1 L_cons%=\n"
        "s_setprio 3\n"
        "s_and_b32 s34, s18, 1\n"
        "s_bfe_u32 s35, s18, 0x10001\n"
        "s_lshl1_add_u32 s34, s34, s35\n"
        "s_lshr_b32 s18, s18, 2\n"
        "s_mul_i32 s35, s34, 26\n"
        "s_add_u32 s35, s35, s19\n"
        "s_and_b32 s36, s35, 3\n"
        "s_lshr_b32 s35, s35, 2\n"
        "s_mul_i32 s37, s18, 5\n"
        "s_add_u32 s36, s36, s37\n"
        "s_mul_i32 s34, s34, 13\n"
        "s_add_u32 s34, s34, 1\n"
        "s_lshr_b32 s34, s34, 1\n"
        "s_add_u32 s34, s34, s18\n"
        "s_lshl1_add_u32 s34, s19, s34\n"
        "s_sub_u32 s34, s34, 52\n"
        "s_cmp_ge_u32 s19, 26\n"
        "s_cselect_b32 s35, s34, s35\n"
        "s_cselect_b32 s36, 4, s36\n"
        "s_mul_i32 s37, s35, 40\n"
        "v_lshrrev_b32 v12, 3, v1\n"
        "v_and_b32 v13, 7, v1\n"
        "v_lshrrev_b32 v14, 4, v1\n"
        "v_xor_b32 v15, v13, v14\n"
        "v_lshlrev_b32 v15, 4, v15\n"
        "s_lshl_b32 s39, s37, 2\n"
        "s_add_u32 s39, s39, s38\n"
        "s_mul_i32 s39, s39, 1152\n"
        "v_mul_u32_u24 v16, 4608, v12\n"
        "v_add_u32 v16, s39, v16\n"
        "v_add_u32 v2, v16, v15\n"
        "s_mov_b32 s47, 35840\n"
        "v_xor_b32 v15, 64, v15\n"
        "v_add3_u32 v3, v16, v15, s47\n"
        "v_add_u32 v4, 73728, v2\n"
        "v_add_u32 v5, 73728, v3\n"
        "s_lshl_b32 s40, s38, 11\n"
        "s_mov_b32 s43, 0\n"
        "s_mov_b32 s23, 0x20000\n"
        "s_mov_b32 s27, 0x20000\n"
        "s_mov_b32 s22, 4718592\n"
        "s_mov_b32 s26, 1990656\n"
        "s_waitcnt lgkmcnt(0)\n"
        "s_mov_b32 s24, s6\n"
        "s_and_b32 s25, s7, 0xffff\n"
        "s_mov_b32 s20, s4\n"
        "s_and_b32 s21, s5, 0xffff\n"
        "s_add_u32 m0, s40, 73728\n"
        "s_nop 0\n"
        "buffer_load_dwordx4 v2, s[20:23], 0 offen lds\n"
        "buffer_load_dwordx4 v3, s[20:23], 0 offen offset:1024 lds\n"
        "s_cmp_lg_u32 s38, 1\n"
        "s_cbranch_scc1 L_ht0_%=\n"
        "s_mul_i32 s46, s31, 8\n"
        "v_add_u32 v19, s46, v1\n"
        "v_mul_u32_u24 v20, 1821, v19\n"
        "v_lshrrev_b32 v20, 20, v20\n"
        "v_mul_u32_u24 v20, 589824, v20\n"
        "v_lshl_add_u32 v19, v19, 7, v20\n"
        "s_bfm_b64 exec, 8, 0\n"
        "buffer_load_dword v21, v19, s[24:27], 0 offen\n"
        "s_mov_b64 exec, -1\n"
        "L_ht0_%=:\n"
        "s_lshr_b32 s39, s38, 1\n"
        "s_and_b32 s28, s38, 1\n"
        "s_lshl_b32 s41, s39, 2\n"
        "s_add_u32 s41, s41, s37\n"
        "s_add_u32 s41, s41, 32\n"
        "s_lshl_b32 s41, s41, 2\n"
        "s_lshl_b32 s42, s28, 1\n"
        "s_add_u32 s41, s41, s42\n"
        "v_and_b32 v15, 3, v12\n"
        "v_lshrrev_b32 v16, 2, v12\n"
        "v_lshl_add_u32 v15, v15, 2, v16\n"
        "v_add_u32 v15, s41, v15\n"
        "s_mul_i32 s39, s39, 20\n"
        "s_lshl_b32 s42, s28, 3\n"
        "s_add_u32 s39, s39, s42\n"
        "s_add_u32 s39, s39, 132\n"
        "s_lshr_b32 s42, s39, 1\n"
        "v_add_u32 v16, s42, v14\n"
        "v_xor_b32 v16, v16, v13\n"
        "v_and_b32 v16, 7, v16\n"
        "v_lshlrev_b32 v16, 4, v16\n"
        "v_mul_u32_u24 v15, 1152, v15\n"
        "v_add_u32 v6, v15, v16\n"
        "s_lshl_b32 s39, s39, 7\n"
        "s_add_u32 m0, s40, 81920\n"
        "s_nop 0\n"
        "buffer_load_dwordx4 v4, s[20:23], 0 offen lds\n"
        "buffer_load_dwordx4 v5, s[20:23], 0 offen offset:1024 lds\n"
        "v_and_b32 v15, 1, v14\n"
        "v_lshlrev_b32 v15, 1, v15\n"
        "v_xor_b32 v15, v15, v13\n"
        "v_lshlrev_b32 v15, 4, v15\n"
        "s_mul_i32 s41, s38, 18432\n"
        "s_lshl_b32 s42, s36, 7\n"
        "s_add_u32 s41, s41, s42\n"
        "v_mul_u32_u24 v16, 1152, v12\n"
        "v_add_u32 v16, s41, v16\n"
        "v_add_u32 v7, v16, v15\n"
        "s_mov_b32 s47, 8192\n"
        "v_xor_b32 v15, 64, v15\n"
        "v_add3_u32 v8, v16, v15, s47\n"
        "v_add_u32 v9, 663552, v7\n"
        "v_add_u32 v10, 663552, v8\n"
        "v_add_u32 v11, 1327104, v7\n"
        "v_add_u32 v17, 1327104, v8\n"
        "v_mov_b32 v12, v17\n"
        "s_add_u32 m0, s39, 73728\n"
        "s_nop 0\n"
        "buffer_load_dwordx4 v6, s[20:23], 0 offen lds\n"
        "s_add_u32 m0, s40, 0\n"
        "s_nop 0\n"
        "buffer_load_dwordx4 v7, s[24:27], s43 offen lds\n"
        "buffer_load_dwordx4 v8, s[24:27], s43 offen offset:1024 lds\n"
        "s_add_u32 m0, s40, 8192\n"
        "s_nop 0\n"
        "buffer_load_dwordx4 v9, s[24:27], s43 offen lds\n"
        "buffer_load_dwordx4 v10, s[24:27], s43 offen offset:1024 lds\n"
        "s_add_u32 m0, s40, 16384\n"
        "s_nop 0\n"
        "buffer_load_dwordx4 v11, s[24:27], s43 offen lds\n"
        "buffer_load_dwordx4 v12, s[24:27], s43 offen offset:1024 lds\n"
        "s_add_u32 s43, s43, 73728\n"
        "s_lshl_b32 s46, s36, 7\n"
        "v_lshl_add_u32 v14, v1, 1, s46\n"
        "s_lshr_b32 s46, s31, 1\n"
        "s_lshl2_add_u32 s45, s46, s38\n"
        "s_and_b32 s47, s31, 1\n"
        "s_lshl_b32 s47, s47, 6\n"
        "s_add_u32 s84, s45, 0\n"
        "s_mul_i32 s46, s84, 7282\n"
        "s_lshr_b32 s46, s46, 22\n"
        "s_mul_i32 s46, s46, 589824\n"
        "s_lshl_b32 s84, s84, 7\n"
        "s_add_u32 s84, s84, s46\n"
        "s_add_u32 s84, s84, s47\n"
        "s_add_u32 s85, s45, 468\n"
        "s_mul_i32 s46, s85, 7282\n"
        "s_lshr_b32 s46, s46, 22\n"
        "s_mul_i32 s46, s46, 589824\n"
        "s_lshl_b32 s85, s85, 7\n"
        "s_add_u32 s85, s85, s46\n"
        "s_add_u32 s85, s85, s47\n"
        "s_add_u32 s86, s45, 936\n"
        "s_mul_i32 s46, s86, 7282\n"
        "s_lshr_b32 s46, s46, 22\n"
        "s_mul_i32 s46, s46, 589824\n"
        "s_lshl_b32 s86, s86, 7\n"
        "s_add_u32 s86, s86, s46\n"
        "s_add_u32 s86, s86, s47\n"
        "s_add_u32 s87, s45, 1404\n"
        "s_min_u32 s87, s87, 1727\n"
        "s_mul_i32 s46, s87, 7282\n"
        "s_lshr_b32 s46, s46, 22\n"
        "s_mul_i32 s46, s46, 589824\n"
        "s_lshl_b32 s87, s87, 7\n"
        "s_add_u32 s87, s87, s46\n"
        "s_add_u32 s87, s87, s47\n"
        "s_buffer_load_dword s18, s[24:27], s84 offset:73728\n"
        "s_buffer_load_dword s18, s[24:27], s85 offset:73728\n"
        "s_buffer_load_dword s18, s[24:27], s86 offset:73728\n"
        "s_buffer_load_dword s18, s[24:27], s87 offset:73728\n"
        "s_add_u32 m0, s40, 95616\n"
        "s_nop 0\n"
        "buffer_load_dwordx4 v2, s[20:23], 0 offen offset:128 lds\n"
        "s_buffer_load_dword s18, s[24:27], s84 offset:147456\n"
        "s_buffer_load_dword s18, s[24:27], s85 offset:147456\n"
        "s_buffer_load_dword s18, s[24:27], s86 offset:147456\n"
        "s_buffer_load_dword s18, s[24:27], s87 offset:147456\n"
        "buffer_load_dwordx4 v3, s[20:23], 0 offen offset:1152 lds\n"
        "s_add_u32 m0, s40, 103808\n"
        "s_nop 0\n"
        "buffer_load_dwordx4 v4, s[20:23], 0 offen offset:128 lds\n"
        "buffer_load_dwordx4 v5, s[20:23], 0 offen offset:1152 lds\n"
        "s_add_u32 m0, s39, 95616\n"
        "s_nop 0\n"
        "buffer_load_dwordx4 v6, s[20:23], 0 offen offset:128 lds\n"
        "s_buffer_load_dword s18, s[24:27], s84 offset:221184\n"
        "s_buffer_load_dword s18, s[24:27], s85 offset:221184\n"
        "s_buffer_load_dword s18, s[24:27], s86 offset:221184\n"
        "s_buffer_load_dword s18, s[24:27], s87 offset:221184\n"
        "s_add_u32 m0, s40, 24576\n"
        "s_nop 0\n"
        "buffer_load_dwordx4 v7, s[24:27], s43 offen lds\n"
        "buffer_load_dwordx4 v8, s[24:27], s43 offen offset:1024 lds\n"
        "s_add_u32 m0, s40, 32768\n"
        "s_nop 0\n"
        "buffer_load_dwordx4 v9, s[24:27], s43 offen lds\n"
        "buffer_load_dwordx4 v10, s[24:27], s43 offen offset:1024 lds\n"
        "s_add_u32 m0, s40, 40960\n"
        "s_nop 0\n"
        "buffer_load_dwordx4 v11, s[24:27], s43 offen lds\n"
        "buffer_load_dwordx4 v12, s[24:27], s43 offen offset:1024 lds\n"
        "s_add_u32 s43, s43, 73728\n"
        "s_waitcnt vmcnt(11)\n"
        "s_barrier\n"
        "L_ld1%=:\n"
        "s_setprio 0\n"
        "s_buffer_load_dword s18, s[24:27], s84 offset:294912\n"
        "s_buffer_load_dword s18, s[24:27], s85 offset:294912\n"
        "s_buffer_load_dword s18, s[24:27], s86 offset:294912\n"
        "s_buffer_load_dword s18, s[24:27], s87 offset:294912\n"
        "s_add_u32 m0, s40, 117504\n"
        "s_nop 0\n"
        "buffer_load_dwordx4 v2, s[20:23], 0 offen offset:256 lds\n"
        "buffer_load_dwordx4 v3, s[20:23], 0 offen offset:1280 lds\n"
        "s_add_u32 m0, s40, 125696\n"
        "s_nop 0\n"
        "buffer_load_dwordx4 v4, s[20:23], 0 offen offset:256 lds\n"
        "buffer_load_dwordx4 v5, s[20:23], 0 offen offset:1280 lds\n"
        "s_add_u32 m0, s39, 117504\n"
        "s_nop 0\n"
        "buffer_load_dwordx4 v6, s[20:23], 0 offen offset:256 lds\n"
        "s_add_u32 m0, s40, 49152\n"
        "s_nop 0\n"
        "buffer_load_dwordx4 v7, s[24:27], s43 offen lds\n"
        "buffer_load_dwordx4 v8, s[24:27], s43 offen offset:1024 lds\n"
        "s_add_u32 m0, s40, 57344\n"
        "s_nop 0\n"
        "buffer_load_dwordx4 v9, s[24:27], s43 offen lds\n"
        "buffer_load_dwordx4 v10, s[24:27], s43 offen offset:1024 lds\n"
        "s_add_u32 m0, s40, 65536\n"
        "s_nop 0\n"
        "buffer_load_dwordx4 v11, s[24:27], s43 offen lds\n"
        "buffer_load_dwordx4 v12, s[24:27], s43 offen offset:1024 lds\n"
        "s_add_u32 s43, s43, 73728\n"
        "s_waitcnt vmcnt(11)\n"
        "s_barrier\n"
        "s_buffer_load_dword s18, s[24:27], s84 offset:368640\n"
        "s_buffer_load_dword s18, s[24:27], s85 offset:368640\n"
        "s_buffer_load_dword s18, s[24:27], s86 offset:368640\n"
        "s_buffer_load_dword s18, s[24:27], s87 offset:368640\n"
        "s_add_u32 m0, s40, 139392\n"
        "s_nop 0\n"
        "buffer_load_dwordx4 v2, s[20:23], 0 offen offset:384 lds\n"
        "buffer_load_dwordx4 v3, s[20:23], 0 offen offset:1408 lds\n"
        "s_add_u32 m0, s40, 147584\n"
        "s_nop 0\n"
        "buffer_load_dwordx4 v4, s[20:23], 0 offen offset:384 lds\n"
        "buffer_load_dwordx4 v5, s[20:23], 0 offen offset:1408 lds\n"
        "s_add_u32 m0, s39, 139392\n"
        "s_nop 0\n"
        "buffer_load_dwordx4 v6, s[20:23], 0 offen offset:384 lds\n"
        "s_add_u32 m0, s40, 0\n"
        "s_nop 0\n"
        "buffer_load_dwordx4 v7, s[24:27], s43 offen lds\n"
        "buffer_load_dwordx4 v8, s[24:27], s43 offen offset:1024 lds\n"
        "s_add_u32 m0, s40, 8192\n"
        "s_nop 0\n"
        "buffer_load_dwordx4 v9, s[24:27], s43 offen lds\n"
        "buffer_load_dwordx4 v10, s[24:27], s43 offen offset:1024 lds\n"
        "s_add_u32 m0, s40, 16384\n"
        "s_nop 0\n"
        "buffer_load_dwordx4 v11, s[24:27], s43 offen lds\n"
        "buffer_load_dwordx4 v12, s[24:27], s43 offen offset:1024 lds\n"
        "s_add_u32 s43, s43, 73728\n"
        "s_waitcnt vmcnt(11)\n"
        "s_barrier\n"
        "s_buffer_load_dword s18, s[24:27], s84 offset:442368\n"
        "s_buffer_load_dword s18, s[24:27], s85 offset:442368\n"
        "s_buffer_load_dword s18, s[24:27], s86 offset:442368\n"
        "s_buffer_load_dword s18, s[24:27], s87 offset:442368\n"
        "s_add_u32 m0, s40, 73216\n"
        "s_nop 0\n"
        "buffer_load_dwordx4 v2, s[20:23], 0 offen offset:512 lds\n"
        "buffer_load_dwordx4 v3, s[20:23], 0 offen offset:1536 lds\n"
        "s_add_u32 m0, s40, 81408\n"
        "s_nop 0\n"
        "buffer_load_dwordx4 v4, s[20:23], 0 offen offset:512 lds\n"
        "buffer_load_dwordx4 v5, s[20:23], 0 offen offset:1536 lds\n"
        "s_add_u32 m0, s39, 73216\n"
        "s_nop 0\n"
        "buffer_load_dwordx4 v6, s[20:23], 0 offen offset:512 lds\n"
        "s_add_u32 m0, s40, 24576\n"
        "s_nop 0\n"
        "buffer_load_dwordx4 v7, s[24:27], s43 offen lds\n"
        "buffer_load_dwordx4 v8, s[24:27], s43 offen offset:1024 lds\n"
        "s_add_u32 m0, s40, 32768\n"
        "s_nop 0\n"
        "buffer_load_dwordx4 v9, s[24:27], s43 offen lds\n"
        "buffer_load_dwordx4 v10, s[24:27], s43 offen offset:1024 lds\n"
        "s_add_u32 m0, s40, 40960\n"
        "s_nop 0\n"
        "buffer_load_dwordx4 v11, s[24:27], s43 offen lds\n"
        "buffer_load_dwordx4 v12, s[24:27], s43 offen offset:1024 lds\n"
        "s_add_u32 s43, s43, 73728\n"
        "s_waitcnt vmcnt(11)\n"
        "s_barrier\n"
        "s_buffer_load_dword s18, s[24:27], s84 offset:516096\n"
        "s_buffer_load_dword s18, s[24:27], s85 offset:516096\n"
        "s_buffer_load_dword s18, s[24:27], s86 offset:516096\n"
        "s_buffer_load_dword s18, s[24:27], s87 offset:516096\n"
        "s_add_u32 m0, s40, 95104\n"
        "s_nop 0\n"
        "buffer_load_dwordx4 v2, s[20:23], 0 offen offset:640 lds\n"
        "buffer_load_dwordx4 v3, s[20:23], 0 offen offset:1664 lds\n"
        "s_add_u32 m0, s40, 103296\n"
        "s_nop 0\n"
        "buffer_load_dwordx4 v4, s[20:23], 0 offen offset:640 lds\n"
        "buffer_load_dwordx4 v5, s[20:23], 0 offen offset:1664 lds\n"
        "s_add_u32 m0, s39, 95104\n"
        "s_nop 0\n"
        "buffer_load_dwordx4 v6, s[20:23], 0 offen offset:640 lds\n"
        "s_add_u32 m0, s40, 49152\n"
        "s_nop 0\n"
        "buffer_load_dwordx4 v7, s[24:27], s43 offen lds\n"
        "buffer_load_dwordx4 v8, s[24:27], s43 offen offset:1024 lds\n"
        "s_add_u32 m0, s40, 57344\n"
        "s_nop 0\n"
        "buffer_load_dwordx4 v9, s[24:27], s43 offen lds\n"
        "buffer_load_dwordx4 v10, s[24:27], s43 offen offset:1024 lds\n"
        "s_add_u32 m0, s40, 65536\n"
        "s_nop 0\n"
        "buffer_load_dwordx4 v11, s[24:27], s43 offen lds\n"
        "buffer_load_dwordx4 v12, s[24:27], s43 offen offset:1024 lds\n"
        "s_add_u32 s43, s43, 73728\n"
        "s_waitcnt vmcnt(11)\n"
        "s_barrier\n"
        "s_buffer_load_dword s18, s[24:27], s84 offset:589824\n"
        "s_buffer_load_dword s18, s[24:27], s85 offset:589824\n"
        "s_buffer_load_dword s18, s[24:27], s86 offset:589824\n"
        "s_buffer_load_dword s18, s[24:27], s87 offset:589824\n"
        "global_load_ushort v15, v14, s[8:9]\n"
        "global_load_ushort v16, v14, s[10:11]\n"
        "global_load_ushort v17, v14, s[12:13]\n"
        "global_load_ushort v18, v14, s[14:15]\n"
        "s_add_u32 m0, s40, 116992\n"
        "s_nop 0\n"
        "buffer_load_dwordx4 v2, s[20:23], 0 offen offset:768 lds\n"
        "buffer_load_dwordx4 v3, s[20:23], 0 offen offset:1792 lds\n"
        "s_add_u32 m0, s40, 125184\n"
        "s_nop 0\n"
        "buffer_load_dwordx4 v4, s[20:23], 0 offen offset:768 lds\n"
        "buffer_load_dwordx4 v5, s[20:23], 0 offen offset:1792 lds\n"
        "s_add_u32 m0, s39, 116992\n"
        "s_nop 0\n"
        "buffer_load_dwordx4 v6, s[20:23], 0 offen offset:768 lds\n"
        "s_add_u32 m0, s40, 0\n"
        "s_nop 0\n"
        "buffer_load_dwordx4 v7, s[24:27], s43 offen lds\n"
        "buffer_load_dwordx4 v8, s[24:27], s43 offen offset:1024 lds\n"
        "s_add_u32 m0, s40, 8192\n"
        "s_nop 0\n"
        "buffer_load_dwordx4 v9, s[24:27], s43 offen lds\n"
        "buffer_load_dwordx4 v10, s[24:27], s43 offen offset:1024 lds\n"
        "s_add_u32 m0, s40, 16384\n"
        "s_nop 0\n"
        "buffer_load_dwordx4 v11, s[24:27], s43 offen lds\n"
        "buffer_load_dwordx4 v12, s[24:27], s43 offen offset:1024 lds\n"
        "s_add_u32 s43, s43, 73728\n"
        "s_waitcnt vmcnt(15)\n"
        "s_barrier\n"
        "s_add_u32 m0, s40, 138880\n"
        "s_nop 0\n"
        "buffer_load_dwordx4 v2, s[20:23], 0 offen offset:896 lds\n"
        "buffer_load_dwordx4 v3, s[20:23], 0 offen offset:1920 lds\n"
        "s_add_u32 m0, s40, 147072\n"
        "s_nop 0\n"
        "buffer_load_dwordx4 v4, s[20:23], 0 offen offset:896 lds\n"
        "buffer_load_dwordx4 v5, s[20:23], 0 offen offset:1920 lds\n"
        "s_add_u32 m0, s39, 138880\n"
        "s_nop 0\n"
        "buffer_load_dwordx4 v6, s[20:23], 0 offen offset:896 lds\n"
        "s_add_u32 m0, s40, 24576\n"
        "s_nop 0\n"
        "buffer_load_dwordx4 v7, s[24:27], s43 offen lds\n"
        "buffer_load_dwordx4 v8, s[24:27], s43 offen offset:1024 lds\n"
        "s_add_u32 m0, s40, 32768\n"
        "s_nop 0\n"
        "buffer_load_dwordx4 v9, s[24:27], s43 offen lds\n"
        "buffer_load_dwordx4 v10, s[24:27], s43 offen offset:1024 lds\n"
        "s_add_u32 m0, s40, 40960\n"
        "s_nop 0\n"
        "buffer_load_dwordx4 v11, s[24:27], s43 offen lds\n"
        "buffer_load_dwordx4 v12, s[24:27], s43 offen offset:1024 lds\n"
        "s_add_u32 s43, s43, 73728\n"
        "s_waitcnt vmcnt(11)\n"
        "s_barrier\n"
        "s_add_u32 m0, s40, 72704\n"
        "s_nop 0\n"
        "buffer_load_dwordx4 v2, s[20:23], 0 offen offset:1024 lds\n"
        "buffer_load_dwordx4 v3, s[20:23], 0 offen offset:2048 lds\n"
        "s_add_u32 m0, s40, 80896\n"
        "s_nop 0\n"
        "buffer_load_dwordx4 v4, s[20:23], 0 offen offset:1024 lds\n"
        "buffer_load_dwordx4 v5, s[20:23], 0 offen offset:2048 lds\n"
        "s_add_u32 m0, s39, 72704\n"
        "s_nop 0\n"
        "buffer_load_dwordx4 v6, s[20:23], 0 offen offset:1024 lds\n"
        "s_add_u32 m0, s40, 49152\n"
        "s_nop 0\n"
        "buffer_load_dwordx4 v7, s[24:27], s43 offen lds\n"
        "buffer_load_dwordx4 v8, s[24:27], s43 offen offset:1024 lds\n"
        "s_add_u32 m0, s40, 57344\n"
        "s_nop 0\n"
        "buffer_load_dwordx4 v9, s[24:27], s43 offen lds\n"
        "buffer_load_dwordx4 v10, s[24:27], s43 offen offset:1024 lds\n"
        "s_add_u32 m0, s40, 65536\n"
        "s_nop 0\n"
        "buffer_load_dwordx4 v11, s[24:27], s43 offen lds\n"
        "buffer_load_dwordx4 v12, s[24:27], s43 offen offset:1024 lds\n"
        "s_waitcnt vmcnt(11)\n"
        "s_barrier\n"
        "v_lshlrev_b32 v15, 16, v15\n"
        "v_lshlrev_b32 v16, 16, v16\n"
        "v_lshlrev_b32 v17, 16, v17\n"
        "v_lshlrev_b32 v18, 16, v18\n"
        "v_add_f32 v16, 0x3727c5ac, v16\n"
        "v_rsq_f32 v16, v16\n"
        "v_lshlrev_b32 v14, 2, v1\n"
        "v_mul_f32 v17, v17, v16\n"
        "v_fma_f32 v18, -v15, v17, v18\n"
        "v_mul_f32 v17, 0xbfb8aa3b, v17\n"
        "v_mul_f32 v18, 0xbfb8aa3b, v18\n"
        "v_add_u32 v14, 161792, v14\n"
        "s_waitcnt vmcnt(0)\n"
        "ds_write_b32 v14, v17\n"
        "ds_write_b32 v14, v18 offset:256\n"
        "s_waitcnt lgkmcnt(0)\n"
        "s_barrier\n"
        "s_endpgm\n"
        "L_pf%=:\n"
        "s_mul_i32 s34, %1, 187\n"
        "v_add_u32 v1, s34, %2\n"
        "v_mul_u32_u24 v2, 1152, v1\n"
        "v_add_u32 v3, 72576, v2\n"
        "v_add_u32 v4, 145152, v2\n"
        "s_cmp_lg_u32 s44, 0\n"
        "s_cbranch_scc1 L_pfend%=\n"
        "s_bfm_b64 exec, 63, 0\n"
        "s_mov_b32 s23, 0x20000\n"
        "s_mov_b32 s22, 4718592\n"
        "s_waitcnt lgkmcnt(0)\n"
        "s_mov_b32 s20, s4\n"
        "s_and_b32 s21, s5, 0xffff\n"
        "buffer_load_dword v9, v2, s[20:23], 0 offen offset:128\n"
        "buffer_load_dword v9, v3, s[20:23], 0 offen offset:128\n"
        "buffer_load_dword v9, v4, s[20:23], 0 offen offset:128\n"
        "buffer_load_dword v9, v2, s[20:23], 0 offen offset:256\n"
        "buffer_load_dword v9, v3, s[20:23], 0 offen offset:256\n"
        "buffer_load_dword v9, v4, s[20:23], 0 offen offset:256\n"
        "buffer_load_dword v9, v2, s[20:23], 0 offen offset:384\n"
        "buffer_load_dword v9, v3, s[20:23], 0 offen offset:384\n"
        "buffer_load_dword v9, v4, s[20:23], 0 offen offset:384\n"
        "buffer_load_dword v9, v2, s[20:23], 0 offen offset:512\n"
        "buffer_load_dword v9, v3, s[20:23], 0 offen offset:512\n"
        "buffer_load_dword v9, v4, s[20:23], 0 offen offset:512\n"
        "buffer_load_dword v9, v2, s[20:23], 0 offen offset:640\n"
        "buffer_load_dword v9, v3, s[20:23], 0 offen offset:640\n"
        "buffer_load_dword v9, v4, s[20:23], 0 offen offset:640\n"
        "buffer_load_dword v9, v2, s[20:23], 0 offen offset:768\n"
        "buffer_load_dword v9, v3, s[20:23], 0 offen offset:768\n"
        "buffer_load_dword v9, v4, s[20:23], 0 offen offset:768\n"
        "buffer_load_dword v9, v2, s[20:23], 0 offen offset:896\n"
        "buffer_load_dword v9, v3, s[20:23], 0 offen offset:896\n"
        "buffer_load_dword v9, v4, s[20:23], 0 offen offset:896\n"
        "buffer_load_dword v9, v2, s[20:23], 0 offen offset:1024\n"
        "buffer_load_dword v9, v3, s[20:23], 0 offen offset:1024\n"
        "buffer_load_dword v9, v4, s[20:23], 0 offen offset:1024\n"
        "L_pfend%=:\n"
        "s_waitcnt vmcnt(0) lgkmcnt(0)\n"
        "s_endpgm\n"
        ".p2align 12\n"
        "L_cons%=:\n"
        "s_and_b32 s34, s18, 1\n"
        "s_bfe_u32 s35, s18, 0x10001\n"
        "s_lshl1_add_u32 s34, s34, s35\n"
        "s_lshr_b32 s18, s18, 2\n"
        "s_mul_i32 s35, s34, 26\n"
        "s_add_u32 s35, s35, s19\n"
        "s_and_b32 s36, s35, 3\n"
        "s_lshr_b32 s35, s35, 2\n"
        "s_mul_i32 s37, s18, 5\n"
        "s_add_u32 s36, s36, s37\n"
        "s_mul_i32 s34, s34, 13\n"
        "s_add_u32 s34, s34, 1\n"
        "s_lshr_b32 s34, s34, 1\n"
        "s_add_u32 s34, s34, s18\n"
        "s_lshl1_add_u32 s34, s19, s34\n"
        "s_sub_u32 s34, s34, 52\n"
        "s_cmp_ge_u32 s19, 26\n"
        "s_cselect_b32 s35, s34, s35\n"
        "s_cselect_b32 s36, 4, s36\n"
        "s_mul_i32 s37, s35, 40\n"
        "s_getpc_b64 s[46:47]\n"
        "L_pc%=:\n"
        "s_lshl_b32 s45, s38, 6\n"
        "s_add_u32 s45, s45, L_end%=-L_pc%=-2304\n"
        "s_add_u32 s46, s46, s45\n"
        "s_addc_u32 s47, s47, 0\n"
        "s_load_dword s43, s[46:47], 0\n"
        "s_load_dword s43, s[46:47], 256\n"
        "s_load_dword s43, s[46:47], 512\n"
        "s_load_dword s43, s[46:47], 768\n"
        "s_load_dword s43, s[46:47], 1024\n"
        "s_load_dword s43, s[46:47], 1280\n"
        "s_load_dword s43, s[46:47], 1536\n"
        "s_load_dword s43, s[46:47], 1792\n"
        "s_load_dword s43, s[46:47], 2048\n"
        "s_and_b32 s39, s38, 1\n"
        "s_lshr_b32 s45, s38, 1\n"
        "v_and_b32 v2, 15, v1\n"
        "v_lshrrev_b32 v3, 4, v1\n"
        "s_min_u32 s40, s38, 3\n"
        "s_mul_i32 s40, s40, 22016\n"
        "s_add_u32 s40, s40, 73728\n"
        "v_lshlrev_b32 v4, 3, v1\n"
        "v_mov_b32 v6, 0\n"
        "v_mov_b32 v7, 0\n"
        "v_add_u32 v4, s40, v4\n"
        "ds_write_b64 v4, v[6:7] offset:16384\n"
        "ds_write_b64 v4, v[6:7] offset:18944\n"
        "ds_write_b64 v4, v[6:7] offset:21504\n"
        "v_lshrrev_b32 v4, 1, v2\n"
        "v_xor_b32 v4, v4, v3\n"
        "v_lshlrev_b32 v4, 4, v4\n"
        "s_lshl_b32 s40, s45, 13\n"
        "s_add_u32 s40, s40, 73728\n"
        "v_lshl_add_u32 v4, v2, 7, v4\n"
        "v_add_u32 v10, s40, v4\n"
        "v_xor_b32 v11, 64, v10\n"
        "s_mul_i32 s40, s45, 20\n"
        "s_add_u32 s40, s40, 704\n"
        "v_add_u32 v4, s40, v2\n"
        "s_add_u32 s40, s40, 4\n"
        "v_bfe_u32 v5, v4, 1, 3\n"
        "v_xor_b32 v5, v5, v3\n"
        "v_lshlrev_b32 v5, 4, v5\n"
        "v_lshl_add_u32 v12, v4, 7, v5\n"
        "v_xor_b32 v15, 64, v12\n"
        "v_add_u32 v4, s40, v2\n"
        "s_add_u32 s40, s40, 4\n"
        "v_bfe_u32 v5, v4, 1, 3\n"
        "v_xor_b32 v5, v5, v3\n"
        "v_lshlrev_b32 v5, 4, v5\n"
        "v_lshl_add_u32 v13, v4, 7, v5\n"
        "v_xor_b32 v16, 64, v13\n"
        "v_add_u32 v4, s40, v2\n"
        "v_bfe_u32 v5, v4, 1, 3\n"
        "v_xor_b32 v5, v5, v3\n"
        "v_lshlrev_b32 v5, 4, v5\n"
        "v_lshl_add_u32 v14, v4, 7, v5\n"
        "v_xor_b32 v17, 64, v14\n"
        "v_bfe_u32 v4, v1, 2, 2\n"
        "v_lshl_add_u32 v5, v3, 3, v4\n"
        "v_lshrrev_b32 v4, 1, v4\n"
        "v_and_b32 v6, 1, v3\n"
        "v_lshl_or_b32 v4, v6, 1, v4\n"
        "s_lshl_b32 s40, s39, 1\n"
        "v_xor_b32 v4, s40, v4\n"
        "v_lshlrev_b32 v4, 5, v4\n"
        "v_and_b32 v6, 3, v1\n"
        "v_lshl_add_u32 v4, v6, 3, v4\n"
        "v_lshl_add_u32 v18, v5, 7, v4\n"
        "v_xor_b32 v19, 32, v18\n"
        "s_lshl_b32 s40, s36, 7\n"
        "s_lshl_b32 s41, s39, 6\n"
        "s_add_u32 s40, s40, s41\n"
        "v_and_b32 v4, 1, v3\n"
        "v_lshlrev_b32 v4, 5, v4\n"
        "v_lshrrev_b32 v5, 1, v3\n"
        "v_lshl_add_u32 v4, v5, 4, v4\n"
        "v_add_u32 v4, s40, v4\n"
        "s_lshl_b32 s41, s45, 4\n"
        "s_add_u32 s41, s41, s37\n"
        "v_add_u32 v5, s41, v2\n"
        "v_mul_u32_u24 v5, 4608, v5\n"
        "v_add_u32 v20, v5, v4\n"
        "s_lshl_b32 s41, s45, 2\n"
        "s_add_u32 s41, s41, s37\n"
        "s_add_u32 s41, s41, 32\n"
        "v_and_b32 v5, 3, v2\n"
        "v_add_u32 v5, s41, v5\n"
        "v_lshrrev_b32 v6, 2, v2\n"
        "v_lshl_add_u32 v5, v5, 2, v6\n"
        "v_mul_u32_u24 v5, 1152, v5\n"
        "v_add_u32 v21, v5, v4\n"
        "s_lshl_b32 s40, s39, 7\n"
        "s_add_u32 s40, s40, 161792\n"
        "v_lshlrev_b32 v4, 4, v3\n"
        "v_add_u32 v22, s40, v4\n"
        "v_mov_b64 v[24:25], 0\n"
        "v_mov_b64 v[26:27], 0\n"
        "v_mov_b64 v[28:29], 0\n"
        "v_mov_b64 v[30:31], 0\n"
        "v_mov_b64 v[32:33], 0\n"
        "v_mov_b64 v[34:35], 0\n"
        "v_mov_b64 v[36:37], 0\n"
        "v_mov_b64 v[38:39], 0\n"
        "v_mov_b64 v[40:41], 0\n"
        "v_mov_b64 v[42:43], 0\n"
        "v_mov_b64 v[44:45], 0\n"
        "v_mov_b64 v[46:47], 0\n"
        "v_mov_b64 v[48:49], 0\n"
        "v_mov_b64 v[50:51], 0\n"
        "v_mov_b64 v[52:53], 0\n"
        "v_mov_b64 v[54:55], 0\n"
        "v_mov_b64 v[56:57], 0\n"
        "v_mov_b64 v[58:59], 0\n"
        "v_mov_b64 v[60:61], 0\n"
        "v_mov_b64 v[62:63], 0\n"
        "s_waitcnt lgkmcnt(0)\n"
        "s_mov_b32 s28, s16\n"
        "s_and_b32 s29, s17, 0xffff\n"
        "s_mov_b32 s30, 4718592\n"
        "s_mov_b32 s31, 0x20000\n"
        "s_mov_b32 s40, 22016\n"
        "s_mov_b32 s41, 24576\n"
        "s_mov_b32 s42, 8\n"
        "s_mov_b32 s45, 22016\n"
        "s_mov_b32 s43, 4294901248\n"
        "s_mov_b32 s34, 24576\n"
        "s_mov_b32 s35, 4294918144\n"
        "s_movk_i32 s36, 34\n"
        "s_movk_i32 s37, 73\n"
        "s_mov_b32 s47, 0xbfb8aa3b\n"
        "v_mov_b32 v8, s47\n"
        "v_mov_b32 v9, s47\n"
        ".p2align 6\n"
        "s_nop 0\n"
        "s_nop 0\n"
        "s_nop 0\n"
        "s_nop 0\n"
        "s_nop 0\n"
        "s_barrier\n"
        "ds_read_b64_tr_b16 v[92:93], v18 offset:0\n"
        "ds_read_b64_tr_b16 v[94:95], v18 offset:512\n"
        "ds_read_b64_tr_b16 v[96:97], v19 offset:0\n"
        "ds_read_b64_tr_b16 v[98:99], v19 offset:512\n"
        "ds_read_b64_tr_b16 v[100:101], v18 offset:8192\n"
        "ds_read_b64_tr_b16 v[102:103], v18 offset:8704\n"
        "ds_read_b64_tr_b16 v[104:105], v19 offset:8192\n"
        "ds_read_b64_tr_b16 v[106:107], v19 offset:8704\n"
        "ds_read_b64_tr_b16 v[108:109], v18 offset:16384\n"
        "ds_read_b64_tr_b16 v[110:111], v18 offset:16896\n"
        "ds_read_b64_tr_b16 v[112:113], v19 offset:16384\n"
        "ds_read_b64_tr_b16 v[114:115], v19 offset:16896\n"
        "ds_read_b128 v[64:67], v10 offset:0\n"
        "ds_read_b128 v[68:71], v10 offset:2048\n"
        "ds_read_b128 v[72:75], v10 offset:4096\n"
        "ds_read_b128 v[76:79], v10 offset:6144\n"
        "ds_read_b128 v[80:83], v12\n"
        "ds_read_b128 v[84:87], v13\n"
        "ds_read_b128 v[88:91], v14\n"
        "s_nop 0\n"
        "s_waitcnt lgkmcnt(0)\n"
        "L_loop%=:\n"
        "v_mfma_f32_16x16x32_bf16 v[32:35], v[92:95], v[64:67], v[32:35]\n"
        "ds_read_b64_tr_b16 v[144:145], v18 offset:4096\n"
        "v_mfma_f32_16x16x32_bf16 v[40:43], v[92:95], v[68:71], v[40:43]\n"
        "ds_read_b64_tr_b16 v[146:147], v18 offset:4608\n"
        "v_mfma_f32_16x16x32_bf16 v[48:51], v[92:95], v[72:75], v[48:51]\n"
        "ds_read_b64_tr_b16 v[148:149], v19 offset:4096\n"
        "v_mfma_f32_16x16x32_bf16 v[56:59], v[92:95], v[80:83], v[56:59]\n"
        "ds_read_b64_tr_b16 v[150:151], v19 offset:4608\n"
        "v_mfma_f32_16x16x32_bf16 v[36:39], v[96:99], v[64:67], v[36:39]\n"
        "ds_read_b64_tr_b16 v[152:153], v18 offset:12288\n"
        "v_mfma_f32_16x16x32_bf16 v[44:47], v[96:99], v[68:71], v[44:47]\n"
        "ds_read_b64_tr_b16 v[154:155], v18 offset:12800\n"
        "v_mfma_f32_16x16x32_bf16 v[52:55], v[96:99], v[72:75], v[52:55]\n"
        "ds_read_b64_tr_b16 v[156:157], v19 offset:12288\n"
        "v_mfma_f32_16x16x32_bf16 v[60:63], v[96:99], v[80:83], v[60:63]\n"
        "ds_read_b64_tr_b16 v[158:159], v19 offset:12800\n"
        "v_mfma_f32_16x16x32_bf16 v[24:27], v[100:103], v[64:67], v[24:27]\n"
        "ds_read_b64_tr_b16 v[160:161], v18 offset:20480\n"
        "v_mfma_f32_16x16x32_bf16 v[32:35], v[100:103], v[68:71], v[32:35]\n"
        "ds_read_b64_tr_b16 v[162:163], v18 offset:20992\n"
        "v_mfma_f32_16x16x32_bf16 v[40:43], v[100:103], v[72:75], v[40:43]\n"
        "ds_read_b64_tr_b16 v[164:165], v19 offset:20480\n"
        "v_mfma_f32_16x16x32_bf16 v[48:51], v[100:103], v[76:79], v[48:51]\n"
        "ds_read_b64_tr_b16 v[166:167], v19 offset:20992\n"
        "v_mfma_f32_16x16x32_bf16 v[56:59], v[100:103], v[84:87], v[56:59]\n"
        "ds_read_b128 v[116:119], v11 offset:0\n"
        "v_mfma_f32_16x16x32_bf16 v[28:31], v[104:107], v[64:67], v[28:31]\n"
        "ds_read_b128 v[120:123], v11 offset:2048\n"
        "v_mfma_f32_16x16x32_bf16 v[36:39], v[104:107], v[68:71], v[36:39]\n"
        "ds_read_b128 v[124:127], v11 offset:4096\n"
        "v_mfma_f32_16x16x32_bf16 v[44:47], v[104:107], v[72:75], v[44:47]\n"
        "ds_read_b128 v[128:131], v11 offset:6144\n"
        "v_mfma_f32_16x16x32_bf16 v[52:55], v[104:107], v[76:79], v[52:55]\n"
        "ds_read_b128 v[132:135], v15\n"
        "v_mfma_f32_16x16x32_bf16 v[60:63], v[104:107], v[84:87], v[60:63]\n"
        "ds_read_b128 v[136:139], v16\n"
        "v_mfma_f32_16x16x32_bf16 v[24:27], v[108:111], v[68:71], v[24:27]\n"
        "ds_read_b128 v[140:143], v17\n"
        "v_mfma_f32_16x16x32_bf16 v[32:35], v[108:111], v[72:75], v[32:35]\n"
        "v_add_u32 v10, s40, v10\n"
        "v_add_u32 v12, s40, v12\n"
        "v_mfma_f32_16x16x32_bf16 v[40:43], v[108:111], v[76:79], v[40:43]\n"
        "v_add_u32 v13, s40, v13\n"
        "v_add_u32 v14, s40, v14\n"
        "v_mfma_f32_16x16x32_bf16 v[56:59], v[108:111], v[88:91], v[56:59]\n"
        "v_add_u32 v18, s41, v18\n"
        "v_add_u32 v19, s41, v19\n"
        "v_mfma_f32_16x16x32_bf16 v[28:31], v[112:115], v[68:71], v[28:31]\n"
        "s_cmp_eq_u32 s42, 1\n"
        "v_mfma_f32_16x16x32_bf16 v[36:39], v[112:115], v[72:75], v[36:39]\n"
        "v_mfma_f32_16x16x32_bf16 v[44:47], v[112:115], v[76:79], v[44:47]\n"
        "s_waitcnt lgkmcnt(0)\n"
        "v_mfma_f32_16x16x32_bf16 v[60:63], v[112:115], v[88:91], v[60:63]\n"
        "s_cbranch_scc1 L_last%=\n"
        "s_barrier\n"
        "v_mfma_f32_16x16x32_bf16 v[32:35], v[144:147], v[116:119], v[32:35]\n"
        "ds_read_b64_tr_b16 v[92:93], v18 offset:0\n"
        "v_mfma_f32_16x16x32_bf16 v[40:43], v[144:147], v[120:123], v[40:43]\n"
        "ds_read_b64_tr_b16 v[94:95], v18 offset:512\n"
        "v_mfma_f32_16x16x32_bf16 v[48:51], v[144:147], v[124:127], v[48:51]\n"
        "ds_read_b64_tr_b16 v[96:97], v19 offset:0\n"
        "v_mfma_f32_16x16x32_bf16 v[56:59], v[144:147], v[132:135], v[56:59]\n"
        "ds_read_b64_tr_b16 v[98:99], v19 offset:512\n"
        "v_mfma_f32_16x16x32_bf16 v[36:39], v[148:151], v[116:119], v[36:39]\n"
        "ds_read_b64_tr_b16 v[100:101], v18 offset:8192\n"
        "v_mfma_f32_16x16x32_bf16 v[44:47], v[148:151], v[120:123], v[44:47]\n"
        "ds_read_b64_tr_b16 v[102:103], v18 offset:8704\n"
        "v_mfma_f32_16x16x32_bf16 v[52:55], v[148:151], v[124:127], v[52:55]\n"
        "ds_read_b64_tr_b16 v[104:105], v19 offset:8192\n"
        "v_mfma_f32_16x16x32_bf16 v[60:63], v[148:151], v[132:135], v[60:63]\n"
        "ds_read_b64_tr_b16 v[106:107], v19 offset:8704\n"
        "v_mfma_f32_16x16x32_bf16 v[24:27], v[152:155], v[116:119], v[24:27]\n"
        "ds_read_b64_tr_b16 v[108:109], v18 offset:16384\n"
        "v_mfma_f32_16x16x32_bf16 v[32:35], v[152:155], v[120:123], v[32:35]\n"
        "ds_read_b64_tr_b16 v[110:111], v18 offset:16896\n"
        "v_mfma_f32_16x16x32_bf16 v[40:43], v[152:155], v[124:127], v[40:43]\n"
        "ds_read_b64_tr_b16 v[112:113], v19 offset:16384\n"
        "v_mfma_f32_16x16x32_bf16 v[48:51], v[152:155], v[128:131], v[48:51]\n"
        "ds_read_b64_tr_b16 v[114:115], v19 offset:16896\n"
        "v_mfma_f32_16x16x32_bf16 v[56:59], v[152:155], v[136:139], v[56:59]\n"
        "ds_read_b128 v[64:67], v10 offset:0\n"
        "v_mfma_f32_16x16x32_bf16 v[28:31], v[156:159], v[116:119], v[28:31]\n"
        "ds_read_b128 v[68:71], v10 offset:2048\n"
        "v_mfma_f32_16x16x32_bf16 v[36:39], v[156:159], v[120:123], v[36:39]\n"
        "ds_read_b128 v[72:75], v10 offset:4096\n"
        "v_mfma_f32_16x16x32_bf16 v[44:47], v[156:159], v[124:127], v[44:47]\n"
        "ds_read_b128 v[76:79], v10 offset:6144\n"
        "v_mfma_f32_16x16x32_bf16 v[52:55], v[156:159], v[128:131], v[52:55]\n"
        "ds_read_b128 v[80:83], v12\n"
        "v_mfma_f32_16x16x32_bf16 v[60:63], v[156:159], v[136:139], v[60:63]\n"
        "ds_read_b128 v[84:87], v13\n"
        "v_mfma_f32_16x16x32_bf16 v[24:27], v[160:163], v[120:123], v[24:27]\n"
        "ds_read_b128 v[88:91], v14\n"
        "v_mfma_f32_16x16x32_bf16 v[32:35], v[160:163], v[124:127], v[32:35]\n"
        "v_add_u32 v11, s40, v11\n"
        "v_add_u32 v15, s40, v15\n"
        "v_mfma_f32_16x16x32_bf16 v[40:43], v[160:163], v[128:131], v[40:43]\n"
        "v_add_u32 v16, s40, v16\n"
        "v_add_u32 v17, s40, v17\n"
        "v_mfma_f32_16x16x32_bf16 v[56:59], v[160:163], v[140:143], v[56:59]\n"
        "s_sub_u32 s42, s42, 1\n"
        "s_bitcmp1_b32 s36, s42\n"
        "v_mfma_f32_16x16x32_bf16 v[28:31], v[164:167], v[120:123], v[28:31]\n"
        "s_cselect_b32 s40, s43, s45\n"
        "s_bitcmp1_b32 s37, s42\n"
        "v_mfma_f32_16x16x32_bf16 v[36:39], v[164:167], v[124:127], v[36:39]\n"
        "s_cselect_b32 s41, s35, s34\n"
        "s_nop 0\n"
        "v_mfma_f32_16x16x32_bf16 v[44:47], v[164:167], v[128:131], v[44:47]\n"
        "s_waitcnt lgkmcnt(0)\n"
        "v_mfma_f32_16x16x32_bf16 v[60:63], v[164:167], v[140:143], v[60:63]\n"
        "s_branch L_loop%=\n"
        "L_last%=:\n"
        "s_barrier\n"
        "v_mfma_f32_16x16x32_bf16 v[24:27], v[152:155], v[116:119], v[24:27]\n"
        "ds_read_b64_tr_b16 v[100:101], v18 offset:8192\n"
        "ds_read_b64_tr_b16 v[102:103], v18 offset:8704\n"
        "v_mfma_f32_16x16x32_bf16 v[28:31], v[156:159], v[116:119], v[28:31]\n"
        "ds_read_b128 v[64:67], v10 offset:0\n"
        "ds_read_b64_tr_b16 v[104:105], v19 offset:8192\n"
        "v_mfma_f32_16x16x32_bf16 v[24:27], v[160:163], v[120:123], v[24:27]\n"
        "ds_read_b64_tr_b16 v[106:107], v19 offset:8704\n"
        "ds_read_b64_tr_b16 v[108:109], v18 offset:16384\n"
        "v_mfma_f32_16x16x32_bf16 v[28:31], v[164:167], v[120:123], v[28:31]\n"
        "ds_read_b64_tr_b16 v[110:111], v18 offset:16896\n"
        "ds_read_b128 v[68:71], v10 offset:2048\n"
        "v_mfma_f32_16x16x32_bf16 v[48:51], v[144:147], v[124:127], v[48:51]\n"
        "ds_read_b64_tr_b16 v[112:113], v19 offset:16384\n"
        "ds_read_b64_tr_b16 v[114:115], v19 offset:16896\n"
        "v_mfma_f32_16x16x32_bf16 v[52:55], v[148:151], v[124:127], v[52:55]\n"
        "v_add_u32 v11, s40, v11\n"
        "ds_read_b64_tr_b16 v[204:205], v18 offset:12288\n"
        "v_mfma_f32_16x16x32_bf16 v[48:51], v[152:155], v[128:131], v[48:51]\n"
        "ds_read_b64_tr_b16 v[206:207], v18 offset:12800\n"
        "ds_read_b128 v[168:171], v11 offset:0\n"
        "v_mfma_f32_16x16x32_bf16 v[52:55], v[156:159], v[128:131], v[52:55]\n"
        "ds_read_b64_tr_b16 v[208:209], v19 offset:12288\n"
        "ds_read_b64_tr_b16 v[210:211], v19 offset:12800\n"
        "v_mfma_f32_16x16x32_bf16 v[32:35], v[144:147], v[116:119], v[32:35]\n"
        "ds_read_b64_tr_b16 v[212:213], v18 offset:20480\n"
        "ds_read_b64_tr_b16 v[214:215], v18 offset:20992\n"
        "v_mfma_f32_16x16x32_bf16 v[36:39], v[148:151], v[116:119], v[36:39]\n"
        "ds_read_b128 v[172:175], v11 offset:2048\n"
        "ds_read_b64_tr_b16 v[216:217], v19 offset:20480\n"
        "v_mfma_f32_16x16x32_bf16 v[32:35], v[152:155], v[120:123], v[32:35]\n"
        "ds_read_b64_tr_b16 v[218:219], v19 offset:20992\n"
        "ds_read_b128 v[220:223], v22 offset:0\n"
        "v_mfma_f32_16x16x32_bf16 v[36:39], v[156:159], v[120:123], v[36:39]\n"
        "ds_read_b128 v[224:227], v22 offset:64\n"
        "ds_read_b128 v[228:231], v22 offset:256\n"
        "v_mfma_f32_16x16x32_bf16 v[32:35], v[160:163], v[124:127], v[32:35]\n"
        "ds_read_b128 v[232:235], v22 offset:320\n"
        "ds_read_b64_tr_b16 v[92:93], v18 offset:0\n"
        "v_mfma_f32_16x16x32_bf16 v[36:39], v[164:167], v[124:127], v[36:39]\n"
        "ds_read_b64_tr_b16 v[94:95], v18 offset:512\n"
        "ds_read_b128 v[72:75], v10 offset:4096\n"
        "s_waitcnt lgkmcnt(15)\n"
        "v_mfma_f32_16x16x32_bf16 v[24:27], v[100:103], v[64:67], v[24:27]\n"
        "ds_read_b64_tr_b16 v[96:97], v19 offset:0\n"
        "ds_read_b64_tr_b16 v[98:99], v19 offset:512\n"
        "v_mfma_f32_16x16x32_bf16 v[28:31], v[104:107], v[64:67], v[28:31]\n"
        "ds_read_b128 v[76:79], v10 offset:6144\n"
        "ds_read_b64_tr_b16 v[196:197], v18 offset:4096\n"
        "v_mfma_f32_16x16x32_bf16 v[24:27], v[108:111], v[68:71], v[24:27]\n"
        "ds_read_b64_tr_b16 v[198:199], v18 offset:4608\n"
        "ds_read_b128 v[176:179], v11 offset:4096\n"
        "v_mfma_f32_16x16x32_bf16 v[28:31], v[112:115], v[68:71], v[28:31]\n"
        "ds_read_b64_tr_b16 v[200:201], v19 offset:4096\n"
        "ds_read_b64_tr_b16 v[202:203], v19 offset:4608\n"
        "s_waitcnt lgkmcnt(15)\n"
        "v_mfma_f32_16x16x32_bf16 v[24:27], v[204:207], v[168:171], v[24:27]\n"
        "ds_read_b128 v[180:183], v11 offset:6144\n"
        "v_add_u32 v15, s40, v15\n"
        "v_mfma_f32_16x16x32_bf16 v[28:31], v[208:211], v[168:171], v[28:31]\n"
        "v_add_u32 v16, s40, v16\n"
        "v_add_u32 v17, s40, v17\n"
        "v_mfma_f32_16x16x32_bf16 v[24:27], v[212:215], v[172:175], v[24:27]\n"
        "ds_read_b128 v[80:83], v12\n"
        "ds_read_b128 v[84:87], v13\n"
        "v_mfma_f32_16x16x32_bf16 v[28:31], v[216:219], v[172:175], v[28:31]\n"
        "ds_read_b128 v[88:91], v14\n"
        "ds_read_b128 v[184:187], v15\n"
        "s_waitcnt lgkmcnt(13)\n"
        "v_mfma_f32_16x16x32_bf16 v[48:51], v[92:95], v[72:75], v[48:51]\n"
        "ds_read_b128 v[188:191], v16\n"
        "ds_read_b128 v[192:195], v17\n"
        "s_waitcnt lgkmcnt(13)\n"
        "v_mfma_f32_16x16x32_bf16 v[52:55], v[96:99], v[72:75], v[52:55]\n"
        "s_waitcnt lgkmcnt(12)\n"
        "v_mfma_f32_16x16x32_bf16 v[48:51], v[100:103], v[76:79], v[48:51]\n"
        "v_pk_fma_f32 v[24:25], v[24:25], v[220:221], v[228:229]\n"
        "v_pk_fma_f32 v[26:27], v[26:27], v[222:223], v[230:231]\n"
        "v_pk_fma_f32 v[28:29], v[28:29], v[224:225], v[232:233]\n"
        "v_pk_fma_f32 v[30:31], v[30:31], v[226:227], v[234:235]\n"
        "v_mfma_f32_16x16x32_bf16 v[52:55], v[104:107], v[76:79], v[52:55]\n"
        "v_exp_f32 v236, v24\n"
        "s_waitcnt lgkmcnt(9)\n"
        "v_mfma_f32_16x16x32_bf16 v[48:51], v[196:199], v[176:179], v[48:51]\n"
        "v_exp_f32 v237, v25\n"
        "s_waitcnt lgkmcnt(7)\n"
        "v_mfma_f32_16x16x32_bf16 v[52:55], v[200:203], v[176:179], v[52:55]\n"
        "v_exp_f32 v238, v26\n"
        "s_waitcnt lgkmcnt(6)\n"
        "v_mfma_f32_16x16x32_bf16 v[48:51], v[204:207], v[180:183], v[48:51]\n"
        "v_exp_f32 v239, v27\n"
        "v_mfma_f32_16x16x32_bf16 v[52:55], v[208:211], v[180:183], v[52:55]\n"
        "v_exp_f32 v240, v28\n"
        "v_mfma_f32_16x16x32_bf16 v[32:35], v[92:95], v[64:67], v[32:35]\n"
        "v_exp_f32 v241, v29\n"
        "v_exp_f32 v242, v30\n"
        "v_exp_f32 v243, v31\n"
        "v_pk_fma_f32 v[236:237], v[236:237], v[8:9], v[8:9]\n"
        "v_pk_fma_f32 v[238:239], v[238:239], v[8:9], v[8:9]\n"
        "v_pk_fma_f32 v[240:241], v[240:241], v[8:9], v[8:9]\n"
        "v_pk_fma_f32 v[242:243], v[242:243], v[8:9], v[8:9]\n"
        "v_mfma_f32_16x16x32_bf16 v[36:39], v[96:99], v[64:67], v[36:39]\n"
        "v_rcp_f32 v236, v236\n"
        "v_mfma_f32_16x16x32_bf16 v[32:35], v[100:103], v[68:71], v[32:35]\n"
        "v_rcp_f32 v237, v237\n"
        "v_mfma_f32_16x16x32_bf16 v[36:39], v[104:107], v[68:71], v[36:39]\n"
        "v_rcp_f32 v238, v238\n"
        "v_rcp_f32 v239, v239\n"
        "v_rcp_f32 v240, v240\n"
        "v_rcp_f32 v241, v241\n"
        "v_rcp_f32 v242, v242\n"
        "v_rcp_f32 v243, v243\n"
        "v_pk_mul_f32 v[24:25], v[24:25], v[236:237]\n"
        "v_pk_mul_f32 v[26:27], v[26:27], v[238:239]\n"
        "v_pk_mul_f32 v[28:29], v[28:29], v[240:241]\n"
        "v_pk_mul_f32 v[30:31], v[30:31], v[242:243]\n"
        "v_cvt_pk_bf16_f32 v24, v24, v25\n"
        "v_cvt_pk_bf16_f32 v25, v26, v27\n"
        "v_cvt_pk_bf16_f32 v26, v28, v29\n"
        "v_cvt_pk_bf16_f32 v27, v30, v31\n"
        "s_nop 0\n"
        "v_permlane16_swap_b32 v24, v26\n"
        "v_permlane16_swap_b32 v25, v27\n"
        "buffer_store_dwordx4 v[24:27], v20, s[28:31], 0 offen offset:0 sc0 sc1\n"
        "v_pk_fma_f32 v[48:49], v[48:49], v[220:221], v[228:229]\n"
        "v_pk_fma_f32 v[50:51], v[50:51], v[222:223], v[230:231]\n"
        "v_pk_fma_f32 v[52:53], v[52:53], v[224:225], v[232:233]\n"
        "v_pk_fma_f32 v[54:55], v[54:55], v[226:227], v[234:235]\n"
        "v_mfma_f32_16x16x32_bf16 v[32:35], v[108:111], v[72:75], v[32:35]\n"
        "v_exp_f32 v236, v48\n"
        "v_mfma_f32_16x16x32_bf16 v[36:39], v[112:115], v[72:75], v[36:39]\n"
        "v_exp_f32 v237, v49\n"
        "v_mfma_f32_16x16x32_bf16 v[32:35], v[196:199], v[168:171], v[32:35]\n"
        "v_exp_f32 v238, v50\n"
        "v_mfma_f32_16x16x32_bf16 v[36:39], v[200:203], v[168:171], v[36:39]\n"
        "v_exp_f32 v239, v51\n"
        "v_mfma_f32_16x16x32_bf16 v[32:35], v[204:207], v[172:175], v[32:35]\n"
        "v_exp_f32 v240, v52\n"
        "v_mfma_f32_16x16x32_bf16 v[36:39], v[208:211], v[172:175], v[36:39]\n"
        "v_exp_f32 v241, v53\n"
        "v_exp_f32 v242, v54\n"
        "v_exp_f32 v243, v55\n"
        "v_pk_fma_f32 v[236:237], v[236:237], v[8:9], v[8:9]\n"
        "v_pk_fma_f32 v[238:239], v[238:239], v[8:9], v[8:9]\n"
        "v_pk_fma_f32 v[240:241], v[240:241], v[8:9], v[8:9]\n"
        "v_pk_fma_f32 v[242:243], v[242:243], v[8:9], v[8:9]\n"
        "v_mfma_f32_16x16x32_bf16 v[32:35], v[212:215], v[176:179], v[32:35]\n"
        "v_rcp_f32 v236, v236\n"
        "v_mfma_f32_16x16x32_bf16 v[36:39], v[216:219], v[176:179], v[36:39]\n"
        "v_rcp_f32 v237, v237\n"
        "v_mfma_f32_16x16x32_bf16 v[40:43], v[144:147], v[120:123], v[40:43]\n"
        "v_rcp_f32 v238, v238\n"
        "v_mfma_f32_16x16x32_bf16 v[44:47], v[148:151], v[120:123], v[44:47]\n"
        "v_rcp_f32 v239, v239\n"
        "v_mfma_f32_16x16x32_bf16 v[40:43], v[152:155], v[124:127], v[40:43]\n"
        "v_rcp_f32 v240, v240\n"
        "v_mfma_f32_16x16x32_bf16 v[44:47], v[156:159], v[124:127], v[44:47]\n"
        "v_rcp_f32 v241, v241\n"
        "v_rcp_f32 v242, v242\n"
        "v_rcp_f32 v243, v243\n"
        "v_pk_mul_f32 v[48:49], v[48:49], v[236:237]\n"
        "v_pk_mul_f32 v[50:51], v[50:51], v[238:239]\n"
        "v_pk_mul_f32 v[52:53], v[52:53], v[240:241]\n"
        "v_pk_mul_f32 v[54:55], v[54:55], v[242:243]\n"
        "v_mfma_f32_16x16x32_bf16 v[40:43], v[160:163], v[128:131], v[40:43]\n"
        "v_cvt_pk_bf16_f32 v48, v48, v49\n"
        "v_cvt_pk_bf16_f32 v49, v50, v51\n"
        "v_mfma_f32_16x16x32_bf16 v[44:47], v[164:167], v[128:131], v[44:47]\n"
        "v_cvt_pk_bf16_f32 v50, v52, v53\n"
        "v_cvt_pk_bf16_f32 v51, v54, v55\n"
        "v_mfma_f32_16x16x32_bf16 v[40:43], v[92:95], v[68:71], v[40:43]\n"
        "v_permlane16_swap_b32 v48, v50\n"
        "v_mfma_f32_16x16x32_bf16 v[44:47], v[96:99], v[68:71], v[44:47]\n"
        "v_permlane16_swap_b32 v49, v51\n"
        "buffer_store_dwordx4 v[48:51], v20, s[28:31], 0 offen offset:3456 sc0 sc1\n"
        "v_pk_fma_f32 v[32:33], v[32:33], v[220:221], v[228:229]\n"
        "v_pk_fma_f32 v[34:35], v[34:35], v[222:223], v[230:231]\n"
        "v_pk_fma_f32 v[36:37], v[36:37], v[224:225], v[232:233]\n"
        "v_pk_fma_f32 v[38:39], v[38:39], v[226:227], v[234:235]\n"
        "v_mfma_f32_16x16x32_bf16 v[40:43], v[100:103], v[72:75], v[40:43]\n"
        "v_exp_f32 v236, v32\n"
        "v_mfma_f32_16x16x32_bf16 v[44:47], v[104:107], v[72:75], v[44:47]\n"
        "v_exp_f32 v237, v33\n"
        "v_mfma_f32_16x16x32_bf16 v[40:43], v[108:111], v[76:79], v[40:43]\n"
        "v_exp_f32 v238, v34\n"
        "v_mfma_f32_16x16x32_bf16 v[44:47], v[112:115], v[76:79], v[44:47]\n"
        "v_exp_f32 v239, v35\n"
        "v_mfma_f32_16x16x32_bf16 v[40:43], v[196:199], v[172:175], v[40:43]\n"
        "v_exp_f32 v240, v36\n"
        "v_mfma_f32_16x16x32_bf16 v[44:47], v[200:203], v[172:175], v[44:47]\n"
        "v_exp_f32 v241, v37\n"
        "v_exp_f32 v242, v38\n"
        "v_exp_f32 v243, v39\n"
        "v_pk_fma_f32 v[236:237], v[236:237], v[8:9], v[8:9]\n"
        "v_pk_fma_f32 v[238:239], v[238:239], v[8:9], v[8:9]\n"
        "v_pk_fma_f32 v[240:241], v[240:241], v[8:9], v[8:9]\n"
        "v_pk_fma_f32 v[242:243], v[242:243], v[8:9], v[8:9]\n"
        "v_mfma_f32_16x16x32_bf16 v[40:43], v[204:207], v[176:179], v[40:43]\n"
        "v_rcp_f32 v236, v236\n"
        "v_mfma_f32_16x16x32_bf16 v[44:47], v[208:211], v[176:179], v[44:47]\n"
        "v_rcp_f32 v237, v237\n"
        "v_mfma_f32_16x16x32_bf16 v[40:43], v[212:215], v[180:183], v[40:43]\n"
        "v_rcp_f32 v238, v238\n"
        "v_mfma_f32_16x16x32_bf16 v[44:47], v[216:219], v[180:183], v[44:47]\n"
        "v_rcp_f32 v239, v239\n"
        "v_mfma_f32_16x16x32_bf16 v[56:59], v[144:147], v[132:135], v[56:59]\n"
        "v_rcp_f32 v240, v240\n"
        "v_mfma_f32_16x16x32_bf16 v[60:63], v[148:151], v[132:135], v[60:63]\n"
        "v_rcp_f32 v241, v241\n"
        "v_rcp_f32 v242, v242\n"
        "v_rcp_f32 v243, v243\n"
        "v_pk_mul_f32 v[32:33], v[32:33], v[236:237]\n"
        "v_pk_mul_f32 v[34:35], v[34:35], v[238:239]\n"
        "v_pk_mul_f32 v[36:37], v[36:37], v[240:241]\n"
        "v_pk_mul_f32 v[38:39], v[38:39], v[242:243]\n"
        "v_mfma_f32_16x16x32_bf16 v[56:59], v[152:155], v[136:139], v[56:59]\n"
        "v_cvt_pk_bf16_f32 v32, v32, v33\n"
        "v_cvt_pk_bf16_f32 v33, v34, v35\n"
        "v_mfma_f32_16x16x32_bf16 v[60:63], v[156:159], v[136:139], v[60:63]\n"
        "v_cvt_pk_bf16_f32 v34, v36, v37\n"
        "v_cvt_pk_bf16_f32 v35, v38, v39\n"
        "v_mfma_f32_16x16x32_bf16 v[56:59], v[160:163], v[140:143], v[56:59]\n"
        "v_permlane16_swap_b32 v32, v34\n"
        "v_mfma_f32_16x16x32_bf16 v[60:63], v[164:167], v[140:143], v[60:63]\n"
        "v_permlane16_swap_b32 v33, v35\n"
        "buffer_store_dwordx4 v[32:35], v20, s[28:31], 0 offen offset:1152 sc0 sc1\n"
        "v_pk_fma_f32 v[40:41], v[40:41], v[220:221], v[228:229]\n"
        "v_pk_fma_f32 v[42:43], v[42:43], v[222:223], v[230:231]\n"
        "v_pk_fma_f32 v[44:45], v[44:45], v[224:225], v[232:233]\n"
        "v_pk_fma_f32 v[46:47], v[46:47], v[226:227], v[234:235]\n"
        "s_waitcnt lgkmcnt(5)\n"
        "v_mfma_f32_16x16x32_bf16 v[56:59], v[92:95], v[80:83], v[56:59]\n"
        "v_exp_f32 v236, v40\n"
        "v_mfma_f32_16x16x32_bf16 v[60:63], v[96:99], v[80:83], v[60:63]\n"
        "v_exp_f32 v237, v41\n"
        "s_waitcnt lgkmcnt(4)\n"
        "v_mfma_f32_16x16x32_bf16 v[56:59], v[100:103], v[84:87], v[56:59]\n"
        "v_exp_f32 v238, v42\n"
        "v_mfma_f32_16x16x32_bf16 v[60:63], v[104:107], v[84:87], v[60:63]\n"
        "v_exp_f32 v239, v43\n"
        "s_waitcnt lgkmcnt(3)\n"
        "v_mfma_f32_16x16x32_bf16 v[56:59], v[108:111], v[88:91], v[56:59]\n"
        "v_exp_f32 v240, v44\n"
        "v_mfma_f32_16x16x32_bf16 v[60:63], v[112:115], v[88:91], v[60:63]\n"
        "v_exp_f32 v241, v45\n"
        "v_exp_f32 v242, v46\n"
        "v_exp_f32 v243, v47\n"
        "v_pk_fma_f32 v[236:237], v[236:237], v[8:9], v[8:9]\n"
        "v_pk_fma_f32 v[238:239], v[238:239], v[8:9], v[8:9]\n"
        "v_pk_fma_f32 v[240:241], v[240:241], v[8:9], v[8:9]\n"
        "v_pk_fma_f32 v[242:243], v[242:243], v[8:9], v[8:9]\n"
        "s_waitcnt lgkmcnt(2)\n"
        "v_mfma_f32_16x16x32_bf16 v[56:59], v[196:199], v[184:187], v[56:59]\n"
        "v_rcp_f32 v236, v236\n"
        "v_mfma_f32_16x16x32_bf16 v[60:63], v[200:203], v[184:187], v[60:63]\n"
        "v_rcp_f32 v237, v237\n"
        "s_waitcnt lgkmcnt(1)\n"
        "v_mfma_f32_16x16x32_bf16 v[56:59], v[204:207], v[188:191], v[56:59]\n"
        "v_rcp_f32 v238, v238\n"
        "v_mfma_f32_16x16x32_bf16 v[60:63], v[208:211], v[188:191], v[60:63]\n"
        "v_rcp_f32 v239, v239\n"
        "s_waitcnt lgkmcnt(0)\n"
        "v_mfma_f32_16x16x32_bf16 v[56:59], v[212:215], v[192:195], v[56:59]\n"
        "v_rcp_f32 v240, v240\n"
        "v_mfma_f32_16x16x32_bf16 v[60:63], v[216:219], v[192:195], v[60:63]\n"
        "v_rcp_f32 v241, v241\n"
        "v_rcp_f32 v242, v242\n"
        "v_rcp_f32 v243, v243\n"
        "v_pk_mul_f32 v[40:41], v[40:41], v[236:237]\n"
        "v_pk_mul_f32 v[42:43], v[42:43], v[238:239]\n"
        "v_pk_mul_f32 v[44:45], v[44:45], v[240:241]\n"
        "v_pk_mul_f32 v[46:47], v[46:47], v[242:243]\n"
        "v_cvt_pk_bf16_f32 v40, v40, v41\n"
        "v_cvt_pk_bf16_f32 v41, v42, v43\n"
        "v_cvt_pk_bf16_f32 v42, v44, v45\n"
        "v_cvt_pk_bf16_f32 v43, v46, v47\n"
        "s_nop 0\n"
        "v_permlane16_swap_b32 v40, v42\n"
        "v_permlane16_swap_b32 v41, v43\n"
        "buffer_store_dwordx4 v[40:43], v20, s[28:31], 0 offen offset:2304 sc0 sc1\n"
        "v_pk_fma_f32 v[56:57], v[56:57], v[220:221], v[228:229]\n"
        "v_pk_fma_f32 v[58:59], v[58:59], v[222:223], v[230:231]\n"
        "v_pk_fma_f32 v[60:61], v[60:61], v[224:225], v[232:233]\n"
        "v_pk_fma_f32 v[62:63], v[62:63], v[226:227], v[234:235]\n"
        "v_exp_f32 v236, v56\n"
        "v_exp_f32 v237, v57\n"
        "v_exp_f32 v238, v58\n"
        "v_exp_f32 v239, v59\n"
        "v_exp_f32 v240, v60\n"
        "v_exp_f32 v241, v61\n"
        "v_exp_f32 v242, v62\n"
        "v_exp_f32 v243, v63\n"
        "v_pk_fma_f32 v[236:237], v[236:237], v[8:9], v[8:9]\n"
        "v_pk_fma_f32 v[238:239], v[238:239], v[8:9], v[8:9]\n"
        "v_pk_fma_f32 v[240:241], v[240:241], v[8:9], v[8:9]\n"
        "v_pk_fma_f32 v[242:243], v[242:243], v[8:9], v[8:9]\n"
        "v_rcp_f32 v236, v236\n"
        "v_rcp_f32 v237, v237\n"
        "v_rcp_f32 v238, v238\n"
        "v_rcp_f32 v239, v239\n"
        "v_rcp_f32 v240, v240\n"
        "v_rcp_f32 v241, v241\n"
        "v_rcp_f32 v242, v242\n"
        "v_rcp_f32 v243, v243\n"
        "v_pk_mul_f32 v[56:57], v[56:57], v[236:237]\n"
        "v_pk_mul_f32 v[58:59], v[58:59], v[238:239]\n"
        "v_pk_mul_f32 v[60:61], v[60:61], v[240:241]\n"
        "v_pk_mul_f32 v[62:63], v[62:63], v[242:243]\n"
        "v_cvt_pk_bf16_f32 v56, v56, v57\n"
        "v_cvt_pk_bf16_f32 v57, v58, v59\n"
        "v_cvt_pk_bf16_f32 v58, v60, v61\n"
        "v_cvt_pk_bf16_f32 v59, v62, v63\n"
        "s_nop 0\n"
        "v_permlane16_swap_b32 v56, v58\n"
        "v_permlane16_swap_b32 v57, v59\n"
        "buffer_store_dwordx4 v[56:59], v21, s[28:31], 0 offen sc0 sc1\n"
        "L_end%=:\n"
        "s_endpgm\n"
        :: "s"(__builtin_amdgcn_kernarg_segment_ptr()), "s"(blockIdx.x), "v"(threadIdx.x),
           "s"((unsigned)(size_t)(__attribute__((address_space(3))) char*)shm)
        : "s4", "s5", "s6", "s7", "s8", "s9", "s10", "s11", "s12", "s13", "s14", "s15", "s16", "s17", "s18", "s19", "s20", "s21", "s22", "s23", "s24", "s25", "s26", "s27", "s28", "s29", "s30", "s31", "s34", "s35", "s36", "s37", "s38", "s39", "s40", "s41", "s42", "s43", "s44", "s45", "s46", "s47", "s48", "s49", "s50", "s51", "s52", "s53", "s54", "s55", "s56", "s57", "s58", "s59", "s60", "s61", "s62", "s63", "s64", "s65", "s66", "s67", "s68", "s69", "s70", "s71", "s72", "s73", "s74", "s75", "s76", "s77", "s78", "s79", "s80", "s81", "s82", "s83", "s84", "s85", "s86", "s87", "s88", "s89", "s90", "s91", "v1", "v2", "v3", "v4", "v5", "v6", "v7", "v8", "v9", "v10", "v11", "v12", "v13", "v14", "v15", "v16", "v17", "v18", "v19", "v20", "v21", "v22", "v23", "v24", "v25", "v26", "v27", "v28", "v29", "v30", "v31", "v32", "v33", "v34", "v35", "v36", "v37", "v38", "v39", "v40", "v41", "v42", "v43", "v44", "v45", "v46", "v47", "v48", "v49", "v50", "v51", "v52", "v53", "v54", "v55", "v56", "v57", "v58", "v59", "v60", "v61", "v62", "v63", "v64", "v65", "v66", "v67", "v68", "v69", "v70", "v71", "v72", "v73", "v74", "v75", "v76", "v77", "v78", "v79", "v80", "v81", "v82", "v83", "v84", "v85", "v86", "v87", "v88", "v89", "v90", "v91", "v92", "v93", "v94", "v95", "v96", "v97", "v98", "v99", "v100", "v101", "v102", "v103", "v104", "v105", "v106", "v107", "v108", "v109", "v110", "v111", "v112", "v113", "v114", "v115", "v116", "v117", "v118", "v119", "v120", "v121", "v122", "v123", "v124", "v125", "v126", "v127", "v128", "v129", "v130", "v131", "v132", "v133", "v134", "v135", "v136", "v137", "v138", "v139", "v140", "v141", "v142", "v143", "v144", "v145", "v146", "v147", "v148", "v149", "v150", "v151", "v152", "v153", "v154", "v155", "v156", "v157", "v158", "v159", "v160", "v161", "v162", "v163", "v164", "v165", "v166", "v167", "v168", "v169", "v170", "v171", "v172", "v173", "v174", "v175", "v176", "v177", "v178", "v179", "v180", "v181", "v182", "v183", "v184", "v185", "v186", "v187", "v188", "v189", "v190", "v191", "v192", "v193", "v194", "v195", "v196", "v197", "v198", "v199", "v200", "v201", "v202", "v203", "v204", "v205", "v206", "v207", "v208", "v209", "v210", "v211", "v212", "v213", "v214", "v215", "v216", "v217", "v218", "v219", "v220", "v221", "v222", "v223", "v224", "v225", "v226", "v227", "v228", "v229", "v230", "v231", "v232", "v233", "v234", "v235", "v236", "v237", "v238", "v239", "v240", "v241", "v242", "v243", "vcc", "scc", "memory");
}

extern "C" void kernel_launch(void* const* d_in, const int* in_sizes, int n_in, void* d_out, int out_size, void* d_ws,
                              size_t ws_size, hipStream_t stream) {
    conv_bn_silu<<<256, 512, 0, stream>>>(d_in[0], d_in[1], d_in[2], d_in[3], d_in[4], d_in[5], d_out);
}
```
